```python
import torch

def _softmax(x, axis=-1):
    return torch.softmax(x, dim=axis)
def _gelu(x):
    return torch.nn.functional.gelu(x, approximate="tanh")
def _silu(x):
    return torch.nn.functional.silu(x)
def _sigmoid(x):
    return torch.sigmoid(x)
def _layernorm(x, axis=-1, eps=1e-5):
    m = x.mean(dim=axis, keepdim=True)
    v = x.var(dim=axis, keepdim=True, unbiased=False)
    return (x - m) / torch.sqrt(v + eps)
def _scatter_add(tgt, idx, src, axis=0):
    return tgt.clone().index_add_(axis, idx, src)
def _segment_sum(src, seg, n):
    out = torch.zeros((n,) + src.shape[1:], dtype=src.dtype)
    return out.index_add_(0, seg, src)
def _pad(x, pw):
    flat = tuple(v for pair in reversed(pw) for v in pair)
    return torch.nn.functional.pad(x, flat)

kernel_name = 'sequential_preLN__linear_attn_block__dense_activation_mish__prompt'

def setup_inputs():

    _w = (lambda a: (lambda t: t.to(torch.bfloat16) if t.is_floating_point() else t)(torch.as_tensor(a)))
    def _A_setup():
        B, S, D = 1, 2048, 512
        return {
            "x": _w((lambda *s: torch.randn(*s, dtype=torch.bfloat16) * 0.05)(B, S, D)),
            "wqkv": _w((lambda *s: torch.randn(*s, dtype=torch.bfloat16) * 0.05)(D, 3 * D)),
            "wo": _w((lambda *s: torch.randn(*s, dtype=torch.bfloat16) * 0.05)(D, D)),
        }
    _A_in = _A_setup()

    def _B_setup():
        B, S, D, F = 1, 2048, 512, 1792
        return {
            "x": _w((lambda *s: torch.randn(*s, dtype=torch.bfloat16) * 0.05)(B, S, D)),
            "w1": _w((lambda *s: torch.randn(*s, dtype=torch.bfloat16) * 0.8)(D, F) * (D ** -0.5)),
            "b1": _w((lambda *s: torch.randn(*s, dtype=torch.bfloat16) * 0.2)(F)),
            "w2": _w((lambda *s: torch.randn(*s, dtype=torch.bfloat16) * 0.1)(F, D) * (F ** -0.5)),
        }
    _B_in = _B_setup()

    return {
        "A_x": _A_in["x"],
        "A_wqkv": _A_in["wqkv"],
        "A_wo": _A_in["wo"],
        "B_x": _B_in["x"],
        "B_w1": _B_in["w1"],
        "B_b1": _B_in["b1"],
        "B_w2": _B_in["w2"],
    }

def reference(A_x, A_wqkv, A_wo, B_x, B_w1, B_b1, B_w2):
    def _A_ref(x, wqkv, wo):
        B, S, D = x.shape
        H = 32; Dh = D // H
        qkv = (lambda x, *d: x.permute(*d))((x @ wqkv).reshape(B, S, 3, H, Dh), 2, 0, 3, 1, 4)
        q, k, v = qkv[0], qkv[1], qkv[2]
        phi = lambda t: torch.where(t > 0, t + 1, torch.exp(t)) + 1e-6
        pq, pk = phi(q), phi(k)
        kv = torch.einsum("bhsd,bhse->bhde", pk, v)
        z = torch.einsum("bhsd->bhd", pk)
        num = torch.einsum("bhqd,bhde->bhqe", pq, kv)
        den = torch.einsum("bhqd,bhd->bhq", pq, z)[..., None] + 1e-6
        o = num / den
        return (lambda x, *d: x.permute(*d))(o, 0, 2, 1, 3).reshape(B, S, D) @ wo

    def _B_ref(x, w1, b1, w2):
        h = torch.einsum("bsd,df->bsf", x, w1) + b1
        sp = torch.log1p(torch.exp(-torch.abs(h))) + torch.where(h > 0, h, torch.zeros_like(h))
        act = h * torch.tanh(sp)
        return torch.einsum("bsf,fd->bsd", act, w2)

    x = A_x
    h = x + _A_ref(_layernorm(x, axis=-1), A_wqkv, A_wo)
    h2 = h + _B_ref(_layernorm(h, axis=-1), B_w1, B_b1, B_w2)
    return h2
```

```cpp
#include <hip/hip_runtime.h>

typedef unsigned v4u __attribute__((ext_vector_type(4)));
typedef unsigned v2u __attribute__((ext_vector_type(2)));
typedef float f4 __attribute__((ext_vector_type(4)));
typedef float f2 __attribute__((ext_vector_type(2)));
typedef float f16v __attribute__((ext_vector_type(16)));
typedef __bf16 bf8 __attribute__((ext_vector_type(8)));
typedef __bf16 bf2 __attribute__((ext_vector_type(2)));
typedef short s4 __attribute__((ext_vector_type(4)));
#define LAS __attribute__((address_space(3)))
#define GAS __attribute__((address_space(1)))
typedef __amdgpu_buffer_rsrc_t rsrc_t;
typedef LAS char* lptr;

constexpr int SC1 = 16;
constexpr int W_EPOCH = 0;
constexpr int W_FPQ = 0x80000;
constexpr int W_FH = 0xA0000;
constexpr int W_FA = 14 << 20;
constexpr int W_F = 65536;
constexpr int W_FZ = 98304;
constexpr int W_Z1 = 131072;
constexpr int W_HS = 262144;
constexpr int W_P1 = 1 << 20;
constexpr int W_PQ = 3 << 20;
constexpr int W_HB = 5 << 20;
constexpr int W_ACT = 7 << 20;
constexpr int W_END = 15 << 20;
constexpr int W_STAMP = 32 << 20;
constexpr int UB = 16384;
constexpr int L_KVT = 8 * UB, KVS = 544;
constexpr int L_T = L_KVT + 32 * KVS, L_CS = L_T, L_B1 = L_T + 512, L_RS2 = L_T + 6144, L_CNT = L_T + 2048,
              L_DUMMY = L_T + 2304, L_RS = L_T + 2560, L_MR = L_T + 2816, L_CQ = L_T + 3072, L_B1R = L_T + 3584, L_STAMP = L_T + 4096;
constexpr unsigned SPIN_CAP = 1u << 16;
constexpr float LOG2E = 1.4426950408889634f, LN2 = 0.6931471805599453f;
#define MAP 0
#define SLP 2
#define DLY1 20
#define DLY1B 8
#define DLY2 20
#define DLYQ 20
#define LOOK 2
#define SPF 1
#define SPFD 0
#define DLYH 8
#define DLYA 1
#define DLYL 16
#define PUMP 0
#define DLYW 1
#define ROTD 0
#define ROTC 0
#define NW1 0
#define SPOLL 0
#define W1EARLY 1
#define H3U 0
#define PF1 0
#define PF2 0
#define PFD 0
#define TOUCHH 0
#define E1N 28
#define FULLF 1
#define PREW 0
#define C3A 0
#define C3B 0
#define C3C 99
#define T1P 20
#define T2P 10
#define C3D 8
#define YP 8
#define L2A 0
#define CUT0 14
#define CUT1 21
#define CUT2 28
#define CUT3 35
#define CUT4 42
#define XW 12
#define TAFT 1
#define SP1 4
#define SP2 0
#define KVPL 0
#define F1P 0
#define EPOLL 1
#define NPRE 4
#define DLA 12
#define PK1 0
#define PK2 1
#define FLP 11
#define SEQ 1
#define F1S 29
#define WOG0 0
#define PRW 4
#define PQB 1
#define HST 0
#define PQN 16
#define PQW 0
#define DUP1 0
#define FL2 1
#define IDL 100
#define MAPC 0
#define PQR 2
#define DLYB 0
#define ZW 1
#define C3P 0
#define SPN 7
#define CL 8
#define TDL 0
#define MPOS 10
#define PG1 3
#define CODEPF 36608

#define SCHED() __builtin_amdgcn_sched_barrier(0)
#define BAR() do { SCHED(); __builtin_amdgcn_s_waitcnt(0xC07F); asm volatile("s_barrier" ::: "memory"); SCHED(); } while (0)
#define LBAR() do { SCHED(); asm volatile("s_barrier" ::: "memory"); SCHED(); } while (0)
#define WAITV(n) asm volatile("s_waitcnt vmcnt(%0)" ::"n"(n) : "memory")
#define PIN(v) asm volatile("" : "+v"(v))
#define MMA32(acc, a, b) do { acc = mma32(a, b, acc); PIN(acc); SCHED(); } while (0)
#define MMA16(acc, a, b) do { acc = mma16(a, b, acc); PIN(acc); SCHED(); } while (0)
#define DMA(m0v, voff, rs, soff) \
  asm volatile("s_mov_b32 m0, %0\n\ts_nop 0\n\tbuffer_load_dwordx4 %1, %2, %3 offen lds" ::"s"(m0v), "v"(voff), "s"(rs), "s"(soff) : "memory")
#define TOUCH(m0v, voff, rs, soff) \
  asm volatile("s_mov_b32 m0, %0\n\ts_nop 0\n\tbuffer_load_dword %1, %2, %3 offen lds" ::"s"(m0v), "v"(voff), "s"(rs), "s"(soff) : "memory")
#define DMA1(m0v, voff, rs, soff) \
  asm volatile("s_mov_b32 m0, %0\n\ts_nop 0\n\tbuffer_load_dwordx4 %1, %2, %3 offen sc1 lds" ::"s"(m0v), "v"(voff), "s"(rs), "s"(soff) : "memory")
#define ST(k)
#define STR(k)
#define ST_INIT()
#define ST_DUMP()

#define STY(k)
#define STX(k)
__device__ __forceinline__ f4 mma16(v4u a, v4u b, f4 c) {
  return __builtin_amdgcn_mfma_f32_16x16x32_bf16(__builtin_bit_cast(bf8, a), __builtin_bit_cast(bf8, b), c, 0, 0, 0);
}
__device__ __forceinline__ f16v mma32(v4u a, v4u b, f16v c) {
  return __builtin_amdgcn_mfma_f32_32x32x16_bf16(__builtin_bit_cast(bf8, a), __builtin_bit_cast(bf8, b), c, 0, 0, 0);
}
__device__ __forceinline__ unsigned pk(float a, float b) {
  return __builtin_bit_cast(unsigned, __builtin_convertvector(f2{a, b}, bf2));
}
__device__ __forceinline__ float blo(unsigned u) { return __builtin_bit_cast(float, u << 16); }
__device__ __forceinline__ float bhi(unsigned u) { return __builtin_bit_cast(float, u & 0xffff0000u); }
__device__ __forceinline__ v2u trd(lptr p) {
  return __builtin_bit_cast(v2u, __builtin_amdgcn_ds_read_tr16_b64_v4i16((LAS s4*)p));
}
__device__ __forceinline__ v4u trd2(lptr p0, int d) {
  const v2u a = trd(p0), b = trd(p0 + d);
  return v4u{a.x, a.y, b.x, b.y};
}
__device__ __forceinline__ float fu(unsigned u) { return __builtin_bit_cast(float, u); }
__device__ __forceinline__ unsigned uf(float f) { return __builtin_bit_cast(unsigned, f); }
__device__ __forceinline__ unsigned phi2(f2 t) {
  const f2 y = t * LOG2E;
  const f2 e = {__builtin_amdgcn_fmed3f(__builtin_amdgcn_exp2f(y.x), 0.f, 1.f), __builtin_amdgcn_fmed3f(__builtin_amdgcn_exp2f(y.y), 0.f, 1.f)};
  const f2 r = (f2{fmaxf(t.x, 0.f), fmaxf(t.y, 0.f)} + e) + 1e-6f;
  return pk(r.x, r.y);
}
__device__ __forceinline__ float add_x32(float v) {
  const auto s = __builtin_amdgcn_permlane32_swap(uf(v), uf(v), false, false);
  return fu(s[0]) + fu(s[1]);
}
__device__ __forceinline__ float add_x16(float v) {
  const auto s = __builtin_amdgcn_permlane16_swap(uf(v), uf(v), false, false);
  return fu(s[0]) + fu(s[1]);
}
__device__ __forceinline__ v4u ldw(rsrc_t rw, int voff, int e) {
  return __builtin_amdgcn_raw_buffer_load_b128(rw, voff + (e & 3) * 1024, (e >> 2) * 4096, SC1);
}
__device__ __forceinline__ void wait_flags(rsrc_t rw, int vo, unsigned tag) {
#pragma unroll 1
  for (unsigned sp = 0; sp < SPIN_CAP; ++sp) {
    asm volatile("" ::: "memory");
    const unsigned v = __builtin_amdgcn_raw_buffer_load_b32(rw, vo, 0, SC1);
    if (__all(v == tag)) break;
    __builtin_amdgcn_s_sleep(SLP);
  }
  asm volatile("" ::: "memory");
}
template <int N> __device__ __forceinline__ bool flags_s(const char* p, unsigned tag) {
  unsigned v[N];
#pragma unroll
  for (int e = 0; e < N; ++e) asm volatile("s_load_dword %0, %1, %2 glc" : "=s"(v[e]) : "s"(p), "n"(e * 128) : "memory");
  unsigned bad = 0;
#pragma unroll
  for (int e = 0; e < N; ++e) { asm volatile("s_waitcnt lgkmcnt(0)" : "+s"(v[e])::"memory"); bad |= v[e] ^ tag; }
  return bad == 0;
}
template <int N> __device__ __forceinline__ void wait_flags_s(const char* p, unsigned tag) {
#pragma unroll 1
  for (unsigned sp = 0; sp < SPIN_CAP; ++sp)
    if (flags_s<N>(p, tag)) break;
}
__device__ __forceinline__ void set_flag(rsrc_t rw, int off, unsigned tag, int lane) {
  WAITV(0);
  if (lane < 8) __builtin_amdgcn_raw_buffer_store_b128(v4u{tag, tag, tag, tag}, rw, off + lane * 16, 0, SC1);
}
template <int K> __device__ __forceinline__ void spf1(unsigned long long& d, const char* p) {
  asm volatile("s_load_dwordx2 %0, %1, %2" : "+s"(d) : "s"(p), "n"(K * 128 + 60));
}
__device__ __forceinline__ void swait(unsigned long long& d) { asm volatile("s_waitcnt lgkmcnt(0)" : "+s"(d)::"memory"); }
#define SPF1(d, p, k) spf1<(k)>(d, p)
#define SWAIT(d) asm volatile("s_waitcnt lgkmcnt(0)" : "+s"(d)::"memory")
__device__ __forceinline__ void lds_wait(lptr p, unsigned want) {
#pragma unroll 1
  for (unsigned sp = 0; sp < (SPIN_CAP << 4); ++sp) {
    if (__builtin_amdgcn_readfirstlane(*(volatile LAS unsigned*)p) >= want) break;
    __builtin_amdgcn_s_sleep(1);
  }
  asm volatile("" ::: "memory");
}

template <int N> struct IC { static constexpr int value = N; };
template <int A, int B, class F> __device__ __forceinline__ void static_for(F&& f) {
  if constexpr (A < B) { f(IC<A>{}); static_for<A + 1, B>(f); }
}
__device__ __forceinline__ void lds_arrive(lptr p, int lane) {
  __builtin_amdgcn_s_waitcnt(0xC07F);
  asm volatile("" ::: "memory");
  if (lane == 0) __hip_atomic_fetch_add((LAS unsigned*)p, 1u, __ATOMIC_RELAXED, __HIP_MEMORY_SCOPE_WORKGROUP);
}
__device__ __forceinline__ float dot2(unsigned a, unsigned b, float c) {
  return __builtin_amdgcn_fdot2_f32_bf16(__builtin_bit_cast(bf2, a), __builtin_bit_cast(bf2, b), c, false);
}
template <int CTRL> __device__ __forceinline__ float add_dpp(float v) {
  return v + fu(__builtin_amdgcn_update_dpp(0u, uf(v), CTRL, 0xF, 0xF, true));
}

__global__ __launch_bounds__(512, 2) void fused_block(const char* x_, const char* wqkv_, const char* wo_, const char* w1_, const char* b1_,
                                                      const char* w2_, char* out_, char* ws_) {
  __shared__ __attribute__((aligned(16))) char smem[163840];
  lptr L = (lptr)smem;
  const int b = blockIdx.x, tid = threadIdx.x, lane = tid & 63, w = __builtin_amdgcn_readfirstlane(tid >> 6);
  const int i = (b & 7) * 4 + (b >> 6), j = (b >> 3) & 7;
  const int m = (i & 1) * 8 + j, mc = m < 14 ? m : 13, i0 = i & ~1;
  ST_INIT(); STR(62); ST(0);
  typedef unsigned long long u64x8 __attribute__((ext_vector_type(8)));
  u64x8 ka;
  unsigned long long arg, pc;
  unsigned len;
  asm volatile("s_load_dwordx16 %0, %2, 0x0\n\ts_load_dwordx2 %1, %2, %3" : "=&s"(ka), "=&s"(arg) : "s"(__builtin_amdgcn_kernarg_segment_ptr()), "s"(w * 8));
  asm volatile("s_getpc_b64 %0\n1:\n\ts_mov_b32 %1, .Lfb_code_end-1b" : "=s"(pc), "=s"(len));
  const bool G0 = w < 4;
  const int wl = w & 3, rh = wl >> 1, kh = w & 1;
  const int lb = (int)(size_t)L + w * 1024;
  const int lrow = w * 8 + (lane >> 3), lpos = lane & 7;
  const int colb = (lpos ^ (lrow & 2) * 2) * 16;
  const int vq = (lrow - 32) * 3072 + colb;
  int vkv = vq ^ (kh * 32);
  int vx = (lrow - 32) * 1024 + (lpos ^ (4 * kh + (lane >> 4))) * 16;
  const int tn = ((CL << 24 | 0x103040) >> (8 * wl)) & 255;
  int tfirst = (w == 3 ? (b >> 3) & (256 / CL - 1) : w == 0 ? i * 8 + j : b) * tn * 128, tsize = w == 3 ? (int)len : tn * 32768, tv = min(lane, tn - 1) * 128;
  int pfo = (b * 56 + wl * 14) * 128;
  PIN(tv); PIN(vkv); PIN(vx);
  asm volatile("s_waitcnt lgkmcnt(0)" : "+s"(ka), "+s"(arg), "+s"(tfirst), "+s"(tsize), "+s"(pfo));
  const char *x = (const char*)ka[0], *wqkv = (const char*)ka[1], *wo = (const char*)ka[2], *w1 = (const char*)ka[3], *b1 = (const char*)ka[4],
             *w2 = (const char*)ka[5];
  char *out = (char*)ka[6], *ws = (char*)ka[7];
  {
    const rsrc_t rt = __builtin_amdgcn_make_buffer_rsrc((void*)(w == 3 ? pc : arg), 0, tsize, 0x00020000);
    asm volatile("s_cmp_gt_u32 %4, 3\n\ts_cbranch_scc1 2f\n\ts_mov_b32 m0, %0\n\ts_nop 0\n\tbuffer_load_dword %1, %2, %3 offen lds\n2:"
                 ::"s"((int)(size_t)L + L_DUMMY), "v"(tv), "s"(rt), "s"(tfirst), "s"(w) : "memory", "scc");
  }
  unsigned long long sd0 = 0;
  if (SP1 == 3) {
    const char* pf = w1 + (b * 56 + w * 7) * 128;
    static_for<0, SPN>([&](auto N_) { SPF1(sd0, pf, decltype(N_)::value); });
  }
  const int w1c = ZW && m >= 14 ? 0x40000000 : mc * 256;
  const rsrc_t rx = __builtin_amdgcn_make_buffer_rsrc((void*)x, 0, 2048 * 1024, 0x00020000);
  const rsrc_t rq = __builtin_amdgcn_make_buffer_rsrc((void*)wqkv, 0, 512 * 3072, 0x00020000);
  const rsrc_t ro = __builtin_amdgcn_make_buffer_rsrc((void*)wo, 0, 512 * 1024, 0x00020000);
  const rsrc_t r1 = __builtin_amdgcn_make_buffer_rsrc((void*)w1, 0, 512 * 3584, 0x00020000);
  const rsrc_t r2 = __builtin_amdgcn_make_buffer_rsrc((void*)w2, 0, 1792 * 1024, 0x00020000);
  const rsrc_t rw = __builtin_amdgcn_make_buffer_rsrc((void*)ws, 0, W_END, 0x00020000);
  const int vo = lrow * 1024 + colb, v1 = lrow * 3584 + colb;
  const int rd = ROTD == 1 ? (j + 3 * ((i >> 1) & 1)) % 7 : ROTD == 2 ? j % 7 : 0;
  auto upr = [&](int u7) { const int t = u7 + rd; return t >= 7 ? t - 7 : t; };
  auto w2_src = [&](int u) { const int hf = u >= 7 ? 1 : 0; return (2 * upr(u - 7 * hf) + ((i & 1) ^ hf)) * 131072 + j * 128; };
  auto w2_loc = [&](int u) { return (u < 4 ? u : u == 4 || u == 13 ? 8 : u < 9 ? u - 1 : u - 9) * UB; };
  auto w2_unit4 = [&](int u) {
#pragma unroll
    for (int e = 0; e < 4; ++e) DMA(lb + w2_loc(u) + e * 4096, vo, r2, w2_src(u) + e * 32768);
  };
  auto w2_unit2 = [&](int u) {
#pragma unroll
    for (int e = 0; e < 8; ++e) DMA(lb - 2048 + w2_loc(u) + e * 2048, vo - 16384, r2, w2_src(u) + e * 16384);
  };
  auto w2_8 = [&](int u, int e) { DMA(lb + w2_loc(u) + e * 8192, vo, r2, w2_src(u) + e * 65536); };
  const int ldum = (int)(size_t)L + L_DUMMY;
  const unsigned one2 = 0x3F803F80u;
  const int lb4 = lb - 4096;
  const int ph = 0;
  auto x1 = [&](int kc, int e) { DMA(lb4 + kc * 8192 + e * 4096, vx, rx, i * 65536 + e * 32768 + (kc ^ ph) * 128); };
  auto kv1 = [&](int pos, int e) {
    DMA(lb4 + (4 + pos % 5) * UB + e * 4096, vkv, rq, ((pos ^ ph) * 64 + (e & 1) * 32) * 3072 + j * 128 + 1024 + (e >> 1) * 1024);
  };
  auto q1 = [&](int u, int e) { DMA(lb4 + (4 + (u + 3) % 5) * UB + e * 4096, vq, rq, (((2 * u) ^ ph) * 64 + e * 32) * 3072 + j * 128); };
  auto strm = [&](int pos, int e) {
    if (pos >= 8) { if (e < 4) q1(pos - 8, e); }
    else if (pos >= 6) { if (e < 4) kv1(pos, e); }
    else if (e < 2) x1(pos, e);
    else if (e < 6) kv1(pos, e - 2);
    else if (pos >= 4) x1(pos + 2, e - 6);
  };
  auto flat = [&](int n) {
    if (n < 24) strm(n / 6, n % 6); else if (n < 40) strm(4 + ((n - 24) >> 3), n & 7); else strm(6 + ((n - 40) >> 2), n & 3);
  };
  unsigned tag;
  asm volatile("s_load_dword %0, %1, 0x0" : "=s"(tag) : "s"(ws));
  if (!G0) {
    __builtin_amdgcn_s_setprio(PG1);
    if (SP1 == 4) {
      const char* pf = w1 + pfo;
      static_for<0, SPN>([&](auto N_) { SPF1(sd0, pf, decltype(N_)::value); });
    }
#pragma unroll
    for (int n = 0; n < CUT0; ++n) flat(n);
  } else {
    if (w == 2) {
      const rsrc_t rb = __builtin_amdgcn_make_buffer_rsrc((void*)b1, 0, 3584, 0x00020000);
      TOUCH((int)(size_t)L + L_B1R, lane * 4, rb, mc * 256);
    }
    if (SP2 == 3) {
      unsigned long long sd = 0;
      const char* pf = w2 + (b * 56 + w * 14) * 128;
      __builtin_amdgcn_s_sleep(SPFD);
      static_for<0, 14>([&](auto N_) { SPF1(sd, pf, decltype(N_)::value); });
      SWAIT(sd);
    }
    if (tid < 8) *(LAS unsigned*)(L + L_CNT + tid * 4) = 0u;
  }
  if (SP1 != 4) { SWAIT(tag); SWAIT(sd0); tag += 1u; }

  const int c = lane & 15, g = lane >> 4, q = c >> 2, p = lane & 3, hi = lane >> 5, r32 = lane & 31;
  const v4u zu = {0u, 0u, 0u, 0u};
  const f4 z4 = {0.f, 0.f, 0.f, 0.f};
  const f16v z16 = {0.f, 0.f, 0.f, 0.f, 0.f, 0.f, 0.f, 0.f, 0.f, 0.f, 0.f, 0.f, 0.f, 0.f, 0.f, 0.f};
  const int fq = q & 2;
  const unsigned sv = ((g ^ c) & 1) ? 0u : one2;
  const v4u sel = {sv, sv, sv, sv};
  const int m32b = 2 * i + rh;
  const int wbK = ((4 * hi + q) * 128 + (((g & 1) ^ fq) << 5) + p * 8) ^ (kh * 64), wbS = wbK ^ 64;
  v4u w1f[32];
  const int rc = ROTC ? (m >> 1) & 3 : 0;
  auto read_w1 = [&](int u0) {
#pragma unroll
    for (int k = 0; k < 4; ++k) {
      lptr a = L + (u0 + ((k + rc) & 3)) * UB + wbK;
      PIN(a);
#pragma unroll
      for (int s = 0; s < 8; ++s) w1f[8 * k + s] = trd2(a + s * 2048, 1024);
    }
  };
  unsigned hpk[8];

  if (G0) {
    const v4u ones = {one2, one2, one2, one2};
    f4 ak[4] = {z4, z4, z4, z4}, av[4] = {z4, z4, z4, z4}, csk = z4, csv = z4;
    lptr xa0 = L + c * 128 + ((g ^ (c >> 1)) << 4), xa1 = L + ((c * 128 + ((g ^ (c >> 1)) << 4)) ^ 64);
    lptr wa = L + 4 * UB + (8 * g + q) * 128 + ((w ^ (fq | (g & 1))) << 5) + p * 8;
    struct Fr { v4u x[4], k, v; };
    Fr A, B;
    auto step = [&](Fr& N, const Fr& F, lptr xa, lptr wp, bool mul) {
      v2u t0, t1;
      t0 = trd(wp); if (mul) MMA16(csk, ones, F.k);
      t1 = trd(wp + 512); if (mul) MMA16(ak[0], F.x[0], F.k);
      N.k = v4u{t0.x, t0.y, t1.x, t1.y};
      N.x[0] = *(LAS v4u*)xa; if (mul) MMA16(csv, ones, F.v);
      t0 = trd(wp + 8192); if (mul) MMA16(av[0], F.x[0], F.v);
      t1 = trd(wp + 8192 + 512); if (mul) MMA16(ak[1], F.x[1], F.k);
      N.v = v4u{t0.x, t0.y, t1.x, t1.y};
      N.x[1] = *(LAS v4u*)(xa + 2048); if (mul) MMA16(av[1], F.x[1], F.v);
      N.x[2] = *(LAS v4u*)(xa + 4096); if (mul) MMA16(ak[2], F.x[2], F.k);
      N.x[3] = *(LAS v4u*)(xa + 6144); if (mul) MMA16(av[2], F.x[2], F.v);
      if (mul) MMA16(ak[3], F.x[3], F.k);
      if (mul) MMA16(av[3], F.x[3], F.v);
    };
    if (SP1 == 4) { SWAIT(tag); tag += 1u; }
    __builtin_amdgcn_s_setprio(2);
    const int h = 4 * j + w;
    int po = W_P1 + ((h * 8 + (lane >> 3)) * 32 + i) * 256 + (lane & 7) * 16, pz = W_Z1 + (h * 32 + i) * 128 + (g * 2 + c) * 16;
    lptr rsp = L + L_RS + g * 16;
    PIN(po); PIN(pz); PIN(rsp);
    if (C3P < 0) {
      if (w == 0) TOUCH(ldum, ((b >> 3) * 48 + (lane < 48 ? lane : 0)) * 1024 + 384, rq, 0);
      if (w == 1) TOUCH(ldum, ((b >> 3) * 16 + (lane & 15)) * 1024 + 384, ro, 0);
    }
    if (PRW) {
      const unsigned nt = ~tag;
      const int k = lane >> 3, fl = ((2 * i + rh) * 16 + 2 * j + kh) * 128;
      int a = 0x40000000;
      if (k == 0) a = W_FPQ + fl;
      if (k == 1 && w == 0) a = W_F + b * 128;
      if (k == 1 && w == 1 && (b & 7) == ((b >> 3) & 7)) a = W_FZ + (b >> 3) * 128;
      if (PRW > 2 && k == 2) a = W_FH + fl;
      if (PRW > 3 && k >= 4 && m < 14) a = W_FA + (2 * i0 + w) * 8192 + (((m & 1) * 2 + ((k >> 1) & 1)) * 16 + (m >> 1) * 2 + (k & 1)) * 128;
      __builtin_amdgcn_raw_buffer_store_b128(v4u{nt, nt, nt, nt}, rw, a + (lane & 7) * 16, 0, SC1);
    }
    lptr wa4 = wa + 4 * UB;
    PIN(wa); PIN(wa4); PIN(xa0); PIN(xa1);
#pragma unroll
    for (int pos = 0; pos < 8; ++pos) {
      lptr wp = pos == 4 ? wa4 : wa + (pos % 5) * UB;
      if (pos >= 5) BAR(); else LBAR();
      STY(46 + pos);
      step(A, B, xa0 + pos * 8192, wp, pos > 0);
      if (pos == C3P) {
        if (w == 0) TOUCH(ldum, ((b >> 3) * 48 + (lane < 48 ? lane : 0)) * 1024 + 384, rq, 0);
        if (w == 1) TOUCH(ldum, ((b >> 3) * 16 + (lane & 15)) * 1024 + 384, ro, 0);
      }
      step(B, A, xa1 + pos * 8192, wp + 4096, true);
    }
    if (PRW) WAITV(0);
    BAR();
    f4 r4[4], m4[4];
    MMA16(csk, ones, B.k);
    MMA16(csv, ones, B.v);
#pragma unroll
    for (int mt = 0; mt < 4; ++mt) {
      m4[mt] = *(LAS f4*)(rsp + 256 + mt * 64); MMA16(ak[mt], B.x[mt], B.k);
      r4[mt] = *(LAS f4*)(rsp + mt * 64); MMA16(av[mt], B.x[mt], B.v);
    }
    ST(32);
    unsigned pkk[8], pkv[8];
#pragma unroll
    for (int mt = 0; mt < 4; ++mt) {
      if (KVPL) {
        float k_[4], v_[4], e_[4];
#pragma unroll
        for (int r = 0; r < 4; ++r) { k_[r] = m4[mt][r] * csk[0]; PIN(k_[r]); v_[r] = m4[mt][r] * csv[0]; PIN(v_[r]); }
#pragma unroll
        for (int r = 0; r < 4; ++r) { k_[r] = fmaf(ak[mt][r], r4[mt][r], k_[r]); PIN(k_[r]); v_[r] = fmaf(av[mt][r], r4[mt][r], v_[r]); PIN(v_[r]); }
#pragma unroll
        for (int r = 0; r < 4; ++r) { e_[r] = k_[r] * LOG2E; PIN(e_[r]); }
#pragma unroll
        for (int r = 0; r < 4; ++r) { e_[r] = __builtin_amdgcn_fmed3f(__builtin_amdgcn_exp2f(e_[r]), 0.f, 1.f); PIN(e_[r]); k_[r] = fmaxf(k_[r], 0.f); PIN(k_[r]); }
#pragma unroll
        for (int r = 0; r < 4; ++r) { k_[r] += e_[r]; PIN(k_[r]); }
#pragma unroll
        for (int r = 0; r < 4; ++r) { k_[r] += 1e-6f; PIN(k_[r]); }
        pkk[2 * mt] = pk(k_[0], k_[1]); pkk[2 * mt + 1] = pk(k_[2], k_[3]);
        pkv[2 * mt] = pk(v_[0], v_[1]); pkv[2 * mt + 1] = pk(v_[2], v_[3]);
      } else {
      const f4 kk = ak[mt] * r4[mt] + m4[mt] * csk[0], vv = av[mt] * r4[mt] + m4[mt] * csv[0];
      pkk[2 * mt] = phi2(f2{kk[0], kk[1]}); pkk[2 * mt + 1] = phi2(f2{kk[2], kk[3]});
      pkv[2 * mt] = pk(vv[0], vv[1]); pkv[2 * mt + 1] = pk(vv[2], vv[3]);
      }
    }
    const v4u k0 = {pkk[0], pkk[1], pkk[2], pkk[3]}, k1 = {pkk[4], pkk[5], pkk[6], pkk[7]};
    const v4u v0 = {pkv[0], pkv[1], pkv[2], pkv[3]}, v1v = {pkv[4], pkv[5], pkv[6], pkv[7]};
    f4 kv = mma16(k1, v1v, mma16(k0, v0, z4));
    f4 zz = mma16(k1, ones, mma16(k0, ones, z4));
    __builtin_amdgcn_raw_buffer_store_b128(v4u{uf(kv[0]), tag, uf(kv[1]), tag}, rw, po, 0, SC1);
    __builtin_amdgcn_raw_buffer_store_b128(v4u{uf(kv[2]), tag, uf(kv[3]), tag}, rw, po + 128, 0, SC1);
    if (c < 2) {
      const float za = c == 0 ? zz[0] : zz[2], zb = c == 0 ? zz[1] : zz[3];
      __builtin_amdgcn_raw_buffer_store_b128(v4u{uf(za), tag, uf(zb), tag}, rw, pz, 0, SC1);
    }
    ST(33);
    if (TAFT) LBAR();
    if (T1P == 8 && w == 2) TOUCH(ldum, (b * 56 + (lane < 56 ? lane : 0)) * 128, r1, 0);
    if (T2P == 8 && w == 3) TOUCH(ldum, (b * 56 + (lane < 56 ? lane : 0)) * 128, r2, 0);
    if (C3D == 8 && w == 2) TOUCH(ldum, ((b >> 3) * 56 + (lane < 56 ? lane : 0)) * 1024 + 384, r1, 0);
    if (!TAFT) LBAR();
    if (SP2 == 1 && w >= 2) {
      unsigned long long sd = 0;
      const char* pf = w2 + (b * 56 + (w - 2) * 14) * 128;
      static_for<0, 14>([&](auto N_) { SPF1(sd, pf, decltype(N_)::value); });
      if (WOG0) {
        __builtin_amdgcn_s_setprio(0);
#pragma unroll 4
        for (int e = 0; e < 32; ++e) DMA(lb - 2048 + e * 2048, vo - 16384, ro, e * 16384 + j * 128);
        __builtin_amdgcn_s_setprio(2);
      }
      SWAIT(sd);
    }
    const int pb = w == 2 ? b ^ 4 : b;
    if (w == 0 || (DUP1 && w == 2) || (w == 1 && ((b ^ (b >> 3)) & (DUP1 ? 3 : 7)) == 0)) {
      v4u v[8];
      const int so = (w != 1 ? W_P1 + pb * 8192 : W_Z1 + (b >> 3) * 4096) + lane * 16;
      unsigned todo = w != 1 ? 15 : 3;
      float a0 = 0.f, a1 = 0.f;
      __builtin_amdgcn_s_sleep(DLY1);
#pragma unroll 1
      for (unsigned sp = 0; sp < SPIN_CAP && todo; ++sp) {
        asm volatile("" ::: "memory");
#pragma unroll
        for (int k = 0; k < 4; ++k)
          if (todo >> k & 1) { v[2 * k] = ldw(rw, so, 2 * k); v[2 * k + 1] = ldw(rw, so, 2 * k + 1); }
        SCHED();
#pragma unroll
        for (int k = 0; k < 4; ++k)
          if (todo >> k & 1) {
            const unsigned bad = (v[2 * k].y ^ tag) | (v[2 * k].w ^ tag) | (v[2 * k + 1].y ^ tag) | (v[2 * k + 1].w ^ tag);
            if (__all(bad == 0)) todo &= ~(1u << k);
          }
      }
      if (w != 1) {
        a0 = (fu(v[4].x) + fu(v[5].x)) + (fu(v[6].x) + fu(v[7].x)); a1 = (fu(v[4].z) + fu(v[5].z)) + (fu(v[6].z) + fu(v[7].z));
      }
      a0 += (fu(v[0].x) + fu(v[1].x)) + (fu(v[2].x) + fu(v[3].x)); a1 += (fu(v[0].z) + fu(v[1].z)) + (fu(v[2].z) + fu(v[3].z));
      a0 = add_x32(add_x16(a0)); a1 = add_x32(add_x16(a1));
      if (w != 1) {
        const unsigned pv = pk(a0, a1), pw = __builtin_amdgcn_update_dpp(0u, pv, 0x128, 0xF, 0xF, true);
        if (lane < 8) __builtin_amdgcn_raw_buffer_store_b128(v4u{pv, tag, pw, tag}, rw, W_F + pb * 128 + lane * 16, 0, SC1);
        *(volatile LAS unsigned*)(L + L_CNT) = 1u;
      } else {
        a0 = add_dpp<0x128>(a0); a1 = add_dpp<0x128>(a1);
        const unsigned pv = pk(a0, a1), pw = __builtin_amdgcn_update_dpp(0u, pv, 0xB1, 0xF, 0xF, true);
        if (lane < 16 && !(lane & 1)) __builtin_amdgcn_raw_buffer_store_b128(v4u{pv, tag, pw, tag}, rw, W_FZ + (b >> 3) * 128 + lane * 8, 0, SC1);
      }
    }
    {
      v4u v[8], zv;
      lptr kt = L + L_KVT + w * (8 * KVS) + r32 * 16 + hi * 8;
      lptr zt = L + L_KVT + (8 * w + (r32 >> 2)) * KVS + 512 + (lane & 1) * 16 + (lane & 2) * 4;
      int fo = W_F + w * 8192 + lane * 16, zo = W_FZ + w * 1024 + (r32 >> 2) * 128 + (lane & 3) * 16;
      PIN(kt); PIN(zt); PIN(fo); PIN(zo);
      lds_wait(L + L_CNT + 8, 4u);
      if (w) lds_wait(L + L_CNT, 1u);
      ST(34);
      __builtin_amdgcn_s_sleep(DLY2);
      unsigned todo = 31;
#pragma unroll 1
      for (unsigned sp = 0; sp < SPIN_CAP && todo; ++sp) {
        asm volatile("" ::: "memory");
        ST(42 + (sp < 3 ? sp : 3));
#pragma unroll
        for (int k = 0; k < 4; ++k)
          if (todo >> k & 1) { v[2 * k] = ldw(rw, fo, 2 * k); v[2 * k + 1] = ldw(rw, fo, 2 * k + 1); }
        if (todo & 16) zv = __builtin_amdgcn_raw_buffer_load_b128(rw, zo, 0, SC1);
        if (PQW && sp == 0 && lane == 0) __hip_atomic_fetch_add((LAS unsigned*)(L + L_CNT + 12), 1u, __ATOMIC_RELAXED, __HIP_MEMORY_SCOPE_WORKGROUP);
        SCHED();
#pragma unroll
        for (int k = 0; k < 4; ++k)
          if (todo >> k & 1) {
            unsigned bad = 0;
#pragma unroll
            for (int e = 2 * k; e < 2 * k + 2; ++e) {
              bad |= (v[e].y ^ tag) | (v[e].w ^ tag);
              *(LAS v2u*)(kt + e * KVS) = v2u{v[e].x, v[e].z};
            }
            if (__all(bad == 0)) todo &= ~(1u << k);
          }
        if (todo & 16) {
          *(LAS v2u*)zt = v2u{zv.x, zv.z};
          if (__all(((zv.y ^ tag) | (zv.w ^ tag)) == 0)) todo &= 15u;
        }
      }
    }
    ST(35);
    __builtin_amdgcn_s_setprio(0);
    BAR();
    if (DLYB) __builtin_amdgcn_s_sleep(DLYB);
    if (C3D == 9 && w == 2) TOUCH(ldum, ((b >> 3) * 56 + (lane < 56 ? lane : 0)) * 1024 + 384, r1, 0);
    if (T2P == 9 && w == 3) TOUCH(ldum, (b * 56 + (lane < 56 ? lane : 0)) * 128, r2, 0);
    if (b == 0 && tid == 0) __builtin_amdgcn_raw_buffer_store_b32(tag, rw, W_EPOCH, 0, SC1);
#pragma unroll 4
    for (int e = NW1; e < 16; ++e) DMA(lb + 4 * UB + e * 4096, v1, r1, e * (32 * 3584) + w1c);
    if (T2P == 10 && w == 3) TOUCH(ldum, (b * 56 + (lane < 56 ? lane : 0)) * 128, r2, 0);
    if (w < 2) *(LAS float*)(L + L_B1 + tid * 4) = blo(*(LAS unsigned short*)(L + L_B1R + tid * 2)) * LOG2E;
    unsigned long long sd4 = 0;
    if (SP2 == 4) {
      const char* pf = w2 + (b * 56 + w * 14) * 128;
      static_for<0, 14>([&](auto N_) { SPF1(sd4, pf, decltype(N_)::value); });
    }
    LBAR();
#pragma unroll
    for (int e = 0; e < 8; ++e) DMA(lb + (e >> 2) * 32768 + (e & 3) * 4096, v1, r1, ((e >> 2) * 8 + (e & 3)) * (32 * 3584) + w1c + 128);
    ST(36);
    WAITV(8); LBAR();
#pragma unroll
    for (int e = 0; e < 8; ++e) DMA(lb + UB + (e >> 2) * 32768 + (e & 3) * 4096, v1, r1, ((e >> 2) * 8 + 4 + (e & 3)) * (32 * 3584) + w1c + 128);
    if (T2P == 11) TOUCH(ldum, (b * 56 + w * 14 + (lane < 14 ? lane : 0)) * 128, r2, 0);
    SWAIT(sd4);
    read_w1(4);
    WAITV(T2P == 11 ? 1 : 0); LBAR();
    if (C3C == 99 && w == 2) TOUCH(ldum, ((b >> 3) * 56 + (lane < 56 ? lane : 0)) * 1024 + 384, r2, 0);
#pragma unroll
    for (int e = 0; e < 8; ++e) hpk[e] = 0u;
  } else {
    v2u xres[4];
    f16v aq = z16;
    float rstd, mr;
    f4 c4q[4];
    int qo = W_PQ + ((m32b * 32 + 4 * j + 2 * kh) * 64 + lane) * 16;
    const int xrow = rh * 32 + r32, xsw = (r32 >> 1) & 7;
    {
      lptr xq[4];
#pragma unroll
      for (int t = 0; t < 4; ++t) { xq[t] = L + ((xrow * 128 + ((hi ^ xsw) << 4)) ^ (t * 32)); PIN(xq[t]); }
      v4u xf[32];
      const v4u ones = {one2, one2, one2, one2};
      f4 rs = z4, gr = z4;
      lptr xs0 = L + wl * 2048 + c * 128 + ((g ^ (c >> 1)) << 4), xs1 = L + ((wl * 2048 + c * 128 + ((g ^ (c >> 1)) << 4)) ^ 64);
      lptr qa = L + 4 * UB + (8 * hi + q) * 128 + (((2 * kh + (g & 1)) ^ fq) << 5) + p * 8, qa8 = qa + 4 * UB;
      PIN(xs0); PIN(xs1); PIN(qa); PIN(qa8);
      v4u a[4];
      unsigned long long sd = 0;
      const char* pf = w1 + pfo;
      if (SP1 == 2) {
        if (SPFD) __builtin_amdgcn_s_sleep(SPFD);
        static_for<0, 14>([&](auto N_) { SPF1(sd, pf, decltype(N_)::value); });
      }
      static_for<0, 64>([&](auto N_) {
        constexpr int pos = decltype(N_)::value >> 3, e = decltype(N_)::value & 7;
        constexpr int cut[9] = {CUT0, CUT1, CUT2, CUT3, CUT4, 48, 55, 60, 64}, pend[8] = {6, 12, 18, 24, 32, 40, 44, 48};
        if constexpr (e == 0) {
          if (pos == 0) ST(40);
          WAITV(cut[pos] - pend[pos]);
          if (pos == 0) ST(41);
          if (pos == 7) BAR(); else LBAR();
        }
        constexpr int per = cut[pos + 1] - cut[pos] > 8 ? 2 : 1, n0 = cut[pos] + per * e;
        if constexpr (n0 < cut[pos + 1]) flat(n0);
        if constexpr (per == 2 && n0 + 1 < cut[pos + 1]) flat(n0 + 1);
        if constexpr (SP1 == 1 && pos < 2 && e < 7) SPF1(sd, pf, pos * 7 + e);
        if constexpr (SP1 == 4 && pos == 0 && SPN + e < 14) SPF1(sd0, pf, SPN + e);
        if constexpr (pos == 3 && e == 0) { swait(sd); swait(sd0); }
        if constexpr (e < 4 && pos >= 3 && pos <= 6) { MMA16(rs, a[e], ones); MMA16(gr, a[e], a[e]); }
        if constexpr (pos < 6 && (e < 4 || pos >= 4)) {
          constexpr int kc = e < 4 ? pos : pos + 2;
          xf[4 * kc + (e & 3)] = *(LAS v4u*)(xq[e & 3] + kc * 8192);
        }
        if constexpr (pos >= 2 && pos < 6 && e < 4)
          a[e] = *(LAS v4u*)(((e & 1) ? xs1 : xs0) + (pos < 4 ? 2 * (pos - 2) + (e >> 1) : e < 2 ? pos : pos + 2) * 8192);
        SCHED();
        if constexpr (pos == 6 && e == 7) {
          const float s1 = p == 0 ? rs[0] : p == 1 ? rs[1] : p == 2 ? rs[2] : rs[3];
          const float s2 = p == 0 ? gr[0] : p == 1 ? gr[1] : p == 2 ? gr[2] : gr[3];
          const float mean = s1 * (1.f / 512.f);
          const float r = __builtin_amdgcn_rsqf(fmaxf(s2 * (1.f / 512.f) - mean * mean, 0.f) + 1e-5f);
          if (q == g) {
            *(LAS float*)(L + L_RS + (wl * 16 + c) * 4) = r;
            *(LAS float*)(L + L_MR + (wl * 16 + c) * 4) = -mean * r;
          }
        }
      });
      if (SP1 == 4) { SWAIT(tag); tag += 1u; }
#pragma unroll
      for (int t = 0; t < 4; ++t) xres[t] = *(LAS v2u*)(L + (j ^ ph) * 8192 + xrow * 128 + hi * 8 + (((4 * kh + t) ^ xsw) << 4));
      rstd = *(LAS float*)(L + L_RS + xrow * 4); mr = *(LAS float*)(L + L_MR + xrow * 4);
      f4 cq = z4;
      v4u wf[8];
      __builtin_amdgcn_s_setprio(1);
      ST(21);
      WAITV(XW); BAR();
      ST(22);
      lptr cqw = L + L_CQ + wl * 128 + r32 * 4, cqr = L + L_CQ + wl * 128 + hi * 16;
      PIN(cqw); PIN(cqr); PIN(qo);
#pragma unroll
      for (int s = 0; s < 39; ++s) {
        if (s == YP) { WAITV(WOG0 ? 0 : YP / 2); LBAR(); }
        if (s >= 7) MMA32(aq, wf[(s - 7) & 7], xf[s - 7]);
        if (!WOG0 && s < 32 && (s & 1)) DMA(lb4 + (s >> 1) * 4096, vo - 32 * 1024, ro, (s >> 1) * 32768 + j * 128);
        if (s < 32) {
          const int u = s >> 3;
          wf[s & 7] = trd2((u == 1 ? qa8 : qa + (u == 0 ? 3 : u - 2) * UB) + (s & 7) * 2048, 512);
        }
        if (s >= 3 && s < 35) MMA16(cq, sel, wf[(s - 3) & 7]);
        if (s == 35) {
          if (lane == 0) __hip_atomic_fetch_add((LAS unsigned*)(L + L_CNT + 8), 1u, __ATOMIC_RELAXED, __HIP_MEMORY_SCOPE_WORKGROUP);
          *(LAS float*)cqw = ((g & 1) ? cq[1] : cq[0]);
#pragma unroll
          for (int r4 = 0; r4 < 4; ++r4) c4q[r4] = *(LAS f4*)(cqr + r4 * 32);
        }
        if (s == 38) {
#pragma unroll
          for (int r4 = 0; r4 < 4; ++r4) { c4q[r4] *= mr; PIN(c4q[r4]); }
        }
        SCHED();
      }
    }
    ST(1);
    {
      unsigned o[8];
#pragma unroll
      for (int r4 = 0; r4 < 4; ++r4) {
        const f4 t = f4{aq[4 * r4], aq[4 * r4 + 1], aq[4 * r4 + 2], aq[4 * r4 + 3]} * rstd + c4q[r4];
        o[2 * r4] = phi2(f2{t[0], t[1]}); o[2 * r4 + 1] = phi2(f2{t[2], t[3]});
        if (r4 == 1) { __builtin_amdgcn_raw_buffer_store_b128(v4u{o[0], o[1], o[2], o[3]}, rw, qo, 0, SC1); SCHED(); }
      }
      __builtin_amdgcn_raw_buffer_store_b128(v4u{o[4], o[5], o[6], o[7]}, rw, qo + 1024, 0, SC1);
    }
    ST(2);
    unsigned long long sd2 = 0;
    if (SP2 == 1) {
      const char* pf = w2 + (b * 56 + 28 + wl * 7) * 128;
      static_for<0, 7>([&](auto N_) { SPF1(sd2, pf, decltype(N_)::value); });
    }
    if (SP2 == 2) {
      const char* pf = w2 + (b * 56 + wl * 14) * 128;
      static_for<0, 14>([&](auto N_) { SPF1(sd2, pf, decltype(N_)::value); });
    }
    set_flag(rw, W_FPQ + (m32b * 16 + 2 * j + kh) * 128, tag, lane);
    ST(3);
    __builtin_amdgcn_s_sleep(DLYQ);
    wait_flags(rw, W_FPQ + (m32b * 16 + kh * 8 + (lane & 7)) * 128, tag);
    ST(4);
    v4u pq[16];
    int pqo = W_PQ + (m32b * 32 + 16 * kh) * 1024 + lane * 16;
    PIN(pqo);
    if (PQW) lds_wait(L + L_CNT + 12, 4u);
#pragma unroll
    for (int e = 0; e < PQN; ++e) pq[e] = ldw(rw, pqo, e);
    SCHED();
    lptr ka = L + L_KVT + kh * (16 * KVS) + ((g & 1) ? 512 + hi * 16 : (hi * 16 + c) * 16);
    lptr wK = L + kh * 32768 + wbK, wS = L + kh * 32768 + wbS;
    lptr sp = L + (rh ? L_RS2 + kh * 4096 : L_KVT + kh * (16 * KVS)) + lane * 16, rp = L + (rh ? L_RS2 + (kh ^ 1) * 4096 : L_KVT + (kh ^ 1) * (16 * KVS)) + lane * 16;
    int ho = W_HB + ((m32b * 32 + 4 * j + 2 * kh) * 64 + lane) * 16, so = W_HS + (m32b * 16 + 2 * j + kh) * 256 + r32 * 8;
    f16v ci = z16, accK, accS = z16;
#pragma unroll
    for (int r = 8; r < 16; ++r) ci[r] = 1e-6f;
#pragma unroll
    for (int t = 0; t < 4; ++t) {
      accK[4 * t] = blo(xres[t].x); accK[4 * t + 1] = bhi(xres[t].x); accK[4 * t + 2] = blo(xres[t].y); accK[4 * t + 3] = bhi(xres[t].y);
    }
    PIN(ka); PIN(wK); PIN(wS); PIN(ci); PIN(accK); PIN(sp); PIN(rp); PIN(ho); PIN(so);
    if (!PQB) WAITV(0);
    SWAIT(sd2);
    ST(28);
    BAR();
    ST(5);
    {
      v4u kf[3], of[2], wf[2][2];
      f16v nd[2];
      kf[0] = *(LAS v4u*)ka;
      kf[1] = *(LAS v4u*)(ka + KVS);
      nd[0] = mma32(kf[0], pq[0], ci); PIN(nd[0]);
      SCHED();
#pragma unroll
      for (int e = 0; e < 17; ++e) {
        float r, t[8];
        if (e + 1 < 16) { nd[(e + 1) & 1] = mma32(kf[(e + 1) % 3], pq[e + 1], ci); PIN(nd[(e + 1) & 1]); SCHED(); }
        if (e == MPOS) LBAR();
        if (PQR * e + PQN < 16) {
#pragma unroll
          for (int k = 0; k < PQR; ++k) pq[PQR * e + PQN + k] = ldw(rw, pqo, PQR * e + PQN + k);
        }
        if (e + 2 < 16) kf[(e + 2) % 3] = *(LAS v4u*)(ka + (e + 2) * KVS);
        if (e < 16) {
          r = __builtin_amdgcn_rcpf(nd[e & 1][8]); PIN(r);
          wf[e & 1][0] = trd2(wS + e * 2048, 1024);
#pragma unroll
          for (int k = 0; k < 2; ++k) { t[k] = nd[e & 1][k] * r; PIN(t[k]); }
        }
        SCHED();
        if (e >= 1) MMA32(accS, wf[(e - 1) & 1][0], of[(e - 1) & 1]);
        if (e < 16) {
          wf[e & 1][1] = trd2(wK + e * 2048, 1024);
#pragma unroll
          for (int k = 2; k < 6; ++k) { t[k] = nd[e & 1][k] * r; PIN(t[k]); }
        }
        SCHED();
        if (e >= 1) MMA32(accK, wf[(e - 1) & 1][1], of[(e - 1) & 1]);
        if (e < 16) {
#pragma unroll
          for (int k = 6; k < 8; ++k) { t[k] = nd[e & 1][k] * r; PIN(t[k]); }
          of[e & 1] = v4u{pk(t[0], t[1]), pk(t[2], t[3]), pk(t[4], t[5]), pk(t[6], t[7])}; PIN(of[e & 1]);
        }
        if (e + 1 < 16) PIN(nd[(e + 1) & 1]);
        SCHED();
      }
    }
    ST(6);
#pragma unroll
    for (int r4 = 0; r4 < 4; ++r4) *(LAS f4*)(sp + r4 * 1024) = f4{accS[4 * r4], accS[4 * r4 + 1], accS[4 * r4 + 2], accS[4 * r4 + 3]};
    WAITV(0); BAR();
    ST(26);
    {
      f4 o[4];
#pragma unroll
      for (int r4 = 0; r4 < 4; ++r4) o[r4] = *(LAS f4*)(rp + r4 * 1024);
      SCHED();
#pragma unroll
      for (int r4 = 0; r4 < 4; ++r4) {
        o[r4] += f4{accK[4 * r4], accK[4 * r4 + 1], accK[4 * r4 + 2], accK[4 * r4 + 3]};
        hpk[2 * r4] = pk(o[r4][0], o[r4][1]);
        hpk[2 * r4 + 1] = pk(o[r4][2], o[r4][3]);
        if (r4 & 1) __builtin_amdgcn_raw_buffer_store_b128(v4u{hpk[2 * r4 - 2], hpk[2 * r4 - 1], hpk[2 * r4], hpk[2 * r4 + 1]}, rw, ho + (r4 >> 1) * 1024, 0, SC1);
      }
      SCHED();
      float s1a = 0.f, s1b = 0.f, s2a = 0.f, s2b = 0.f;
#pragma unroll
      for (int e = 0; e < 8; e += 2) {
        s1a = dot2(hpk[e], one2, s1a); s2a = dot2(hpk[e], hpk[e], s2a);
        s1b = dot2(hpk[e + 1], one2, s1b); s2b = dot2(hpk[e + 1], hpk[e + 1], s2b);
      }
      const float a1 = add_x32(s1a + s1b), a2 = add_x32(s2a + s2b);
      __builtin_amdgcn_raw_buffer_store_b64(v2u{uf(a1), uf(a2)}, rw, so, 0, SC1);
    }
    ST(27);
    set_flag(rw, W_FH + (m32b * 16 + 2 * j + kh) * 128, tag, lane);
    if (TOUCHH) TOUCH(ldum, (lane & 15) * 128, rw, W_HB + (m32b * 32 + 4 * j + 2 * kh) * 1024);
    ST(7);
    LBAR();
    read_w1(0);
    SCHED();
  }
  const int nq = (w >> 2) * 2 + kh;
  __builtin_amdgcn_s_setprio(0);
  {
    f4 cs = z4;
#pragma unroll
    for (int s = 0; s < 32; ++s) cs = mma16(sel, w1f[s], cs);
    *(LAS float*)(L + L_CS + nq * 128 + r32 * 4) = (g & 1) ? cs[1] : cs[0];
  }
  const int par = m & 1;
  const int efo = W_FA + m32b * 8192 + (((i & 1) * 2 + kh) * 16 + (c < 14 ? c : 0)) * 128;
  int fin = 0;
  unsigned ef = ~tag;
  lptr pS0 = L + kh * 8192 + wbS, pK0 = L + kh * 8192 + wbK;
  PIN(pS0); PIN(pK0);
  v4u wp_[4 * NPRE][2];
  ST(8);
  if (G0) { __builtin_amdgcn_s_sleep(DLYH); wait_flags(rw, W_FH + ((2 * i0 + wl) * 16 + c) * 128, tag); }
  ST(37);
  BAR();
  STX(60);
  {
    const int vl = lane * 16;
    const int ma = 2 * (i0 + par) + rh, mb = 2 * (i0 + (par ^ 1)) + rh;
    const int oob = m < 14 ? 0 : 0x40000000;
    int ao = oob + W_ACT + ((ma * 112 + 8 * m + 2 * nq) * 64 + lane) * 16, ao2 = oob + W_ACT + ((mb * 112 + 8 * m + 2 * nq) * 64 + lane) * 16;
    const int fo = oob + ((par * 2 + (nq >> 1)) * 16 + (m >> 1) * 2 + (nq & 1)) * 128;
    auto h1 = [&](int k, int e) {
      DMA1(lb + (4 + (k & 3)) * UB + e * 8192, vl, rw, (ZW > 1 ? oob : 0) + W_HB + (i0 + (par ^ (k >> 2))) * 65536 + (((k + rc) & 3) * 8 + w) * 1024 + e * 32768);
    };
    auto feed = [&](int t) {
      if (HST) {
        if (t < 4) { h1(2 + (t >> 1), t & 1); return; }
        if (!(t & 1)) return;
        const int n = t >> 1;
        if (n < 6) w2_8((n - 2) >> 1, n & 1);
        else if (n < 10) h1(4 + ((n - 6) >> 1), n & 1);
        else if (n < 14) w2_8(2 + ((n - 10) >> 1), n & 1);
        else if (n < 18) h1(6 + ((n - 14) >> 1), n & 1);
        else if (n < 20) w2_8(4, n & 1);
        else if (n >= 22 && n < 26) w2_8(5 + ((n - 22) >> 1), n & 1);
        else if (n >= 26 && n < 30) w2_8(n < 28 ? 7 : 9, n & 1);
        return;
      }
      if (!(t & 1)) return;
      const int n = t >> 1;
      if (n < 6) w2_8(n >> 1, n & 1);
      else if (n < 10) h1(4 + ((n - 6) >> 1), n & 1);
      else if (n < 14) w2_8(3 + ((n - 10) >> 1), n & 1);
      else if (n < 18) { if (!FL2) h1(6 + ((n - 14) >> 1), n & 1); else if (n < 16) { h1(n - 8, 0); h1(n - 8, 1); } }
      else if (n >= 22 && n < 26) w2_8(5 + ((n - 22) >> 1), n & 1);
      else if (n >= 26 && n < 30) w2_8(n < 28 ? 7 : 9, n & 1);
    };
    v4u hs[4];
    h1(0, 0); h1(0, 1); h1(1, 0); h1(1, 1);
    asm volatile("buffer_load_dwordx4 %0, %2, %3, %4 offen sc1\n\tbuffer_load_dwordx4 %1, %2, %3, %4 offen offset:1024 sc1"
                 : "=&v"(hs[0]), "=&v"(hs[1])
                 : "v"(vl), "s"(rw), "s"(W_HS + (2 * i0 + wl) * 4096 + (w >> 2) * 2048)
                 : "memory");
    if (!HST) { h1(2, 0); h1(2, 1); h1(3, 0); h1(3, 1); }
    lptr hp = L + 4 * UB + rh * 8192 + lane * 16, tb = L + L_CS + nq * 128 + hi * 16;
    lptr sw = L + L_RS2 + w * 256 + c * 16, sr = L + L_RS2 + (2 * par + rh) * 256 + r32 * 8, sr2 = L + L_RS2 + (2 * (par ^ 1) + rh) * 256 + r32 * 8;
    PIN(hp); PIN(tb); PIN(sw); PIN(sr); PIN(sr2);
    STX(61);
    WAITV(HST ? 4 : 8);
    STX(39);
    LBAR();
    ST(9);
    f16v acc0 = z16, acc1 = z16;
    v4u hr[4];
    hr[0] = *(LAS v4u*)hp; hr[1] = *(LAS v4u*)(hp + 1024); hr[2] = *(LAS v4u*)(hp + 2048);
    SCHED();
    f4 ts, yo[4];
    auto offs = [&](int s, float mr_) {
      if (s >= 24 && s < 28) { yo[s - 24] = *(LAS f4*)(tb + 512 + (s - 24) * 32); hs[s - 24] = *(LAS v4u*)(tb + (s - 24) * 32); }
      if (s >= 28) {
#pragma unroll
        for (int r = 0; r < 4; ++r) { float t = fmaf(mr_, fu(hs[s - 28][r]), yo[s - 28][r]); PIN(t); yo[s - 28][r] = t; }
      }
    };
    f2 pa_, pb_, pc_, pd_;
    float rstd, mr, rstd2, mr2;
#pragma unroll
    for (int s = 0; s < 32; ++s) {
      if (s == 5) { STX(46); asm volatile("s_waitcnt vmcnt(%2)" : "+v"(hs[0]), "+v"(hs[1]) : "n"(HST ? 4 : 6) : "memory"); STX(47); LBAR(); STX(48); }
      if (s == 13) { STX(49); WAITV(HST ? 4 : 6); STX(50); BAR(); STX(51); }
      if (s == 29) { STX(52); WAITV(4); STX(53); BAR(); STX(54); }
      MMA32(acc0, w1f[s], hr[s & 3]);
      hr[(s + 3) & 3] = *(LAS v4u*)(hp + (((s + 3) & 31) >> 3) * UB + ((s + 3) & 7) * 1024);
      feed(s);
      if (s == 5) {
#pragma unroll
        for (int r = 0; r < 4; ++r) { ts[r] = fu(hs[0][r]) + fu(hs[1][r]); }
        PIN(ts);
      } else if (s >= 6 && s < 10) {
        ts[s - 6] = add_x32(add_x16(ts[s - 6])); PIN(ts);
      } else if (s == 10) {
        *(LAS f4*)sw = ts;
      } else if (s == 14) {
        pa_ = *(LAS f2*)sr; pb_ = *(LAS f2*)(sr + 1024); pc_ = *(LAS f2*)sr2; pd_ = *(LAS f2*)(sr2 + 1024);
      } else if (s == 18) {
        mr = (pa_.x + pb_.x) * (1.f / 512.f); rstd = (pa_.y + pb_.y) * (1.f / 512.f); PIN(mr); PIN(rstd);
      } else if (s == 19) {
        mr2 = (pc_.x + pd_.x) * (1.f / 512.f); rstd2 = (pc_.y + pd_.y) * (1.f / 512.f); PIN(mr2); PIN(rstd2);
      } else if (s == 20) {
        rstd = __builtin_amdgcn_rsqf(fmaxf(rstd - mr * mr, 0.f) + 1e-5f); PIN(rstd);
      } else if (s == 21) {
        rstd2 = __builtin_amdgcn_rsqf(fmaxf(rstd2 - mr2 * mr2, 0.f) + 1e-5f); PIN(rstd2);
      } else if (s == 22) {
        rstd *= LOG2E; rstd2 *= LOG2E; mr *= -rstd; mr2 *= -rstd2; PIN(rstd); PIN(rstd2); PIN(mr); PIN(mr2);
      } else if (s == 23) {
        PIN(ao); PIN(ao2);
      }
      offs(s, mr);
      SCHED();
    }
    ST(10);
    auto mish16 = [&](const f16v& a, float rs_, int dst, bool packed) {
#pragma unroll
      for (int hh = 0; hh < 2; ++hh) {
        v4u ov;
        if (packed) {
          f2 yy[4], ee[4], aa[4];
#pragma unroll
          for (int k = 0; k < 4; ++k) {
            yy[k] = f2{a[8 * hh + 2 * k], a[8 * hh + 2 * k + 1]} * rs_ + f2{yo[2 * hh + (k >> 1)][2 * (k & 1)], yo[2 * hh + (k >> 1)][2 * (k & 1) + 1]};
            PIN(yy[k]);
          }
#pragma unroll
          for (int k = 0; k < 4; ++k) { ee[k] = f2{__builtin_amdgcn_exp2f(yy[k].x), __builtin_amdgcn_exp2f(yy[k].y)}; PIN(ee[k]); }
#pragma unroll
          for (int k = 0; k < 4; ++k) { aa[k] = ee[k] * 0.5f + 1.f; PIN(aa[k]); }
#pragma unroll
          for (int k = 0; k < 4; ++k) { ee[k] = ee[k] * aa[k] + 1.f; PIN(ee[k]); }
#pragma unroll
          for (int k = 0; k < 4; ++k) { ee[k] = f2{__builtin_amdgcn_rcpf(ee[k].x), __builtin_amdgcn_rcpf(ee[k].y)}; PIN(ee[k]); }
#pragma unroll
          for (int k = 0; k < 4; ++k) { yy[k] = yy[k] - yy[k] * ee[k]; PIN(yy[k]); }
          ov = v4u{pk(yy[0].x, yy[0].y), pk(yy[1].x, yy[1].y), pk(yy[2].x, yy[2].y), pk(yy[3].x, yy[3].y)};
        } else {
          float y[8], e[8], a_[8];
#pragma unroll
          for (int k = 0; k < 8; ++k) { y[k] = fmaf(a[8 * hh + k], rs_, yo[2 * hh + (k >> 2)][k & 3]); PIN(y[k]); }
#pragma unroll
          for (int k = 0; k < 8; ++k) { e[k] = __builtin_amdgcn_exp2f(y[k]); PIN(e[k]); }
#pragma unroll
          for (int k = 0; k < 8; ++k) { a_[k] = fmaf(e[k], 0.5f, 1.f); PIN(a_[k]); }
#pragma unroll
          for (int k = 0; k < 8; ++k) { e[k] = fmaf(e[k], a_[k], 1.f); PIN(e[k]); }
#pragma unroll
          for (int k = 0; k < 8; ++k) { e[k] = __builtin_amdgcn_rcpf(e[k]); PIN(e[k]); }
#pragma unroll
          for (int k = 0; k < 8; ++k) { y[k] = fmaf(-y[k], e[k], y[k]); PIN(y[k]); }
          ov = v4u{pk(y[0], y[1]), pk(y[2], y[3]), pk(y[4], y[5]), pk(y[6], y[7])};
        }
        PIN(ov);
        SCHED();
        __builtin_amdgcn_raw_buffer_store_b128(ov, rw, dst + hh * 1024, 0, SC1);
        SCHED();
      }
    };
    unsigned o[4];
    float y[4], u1[4];
    float a1[4];
    auto epi = [&](int st, int r4) {
      if (st == 0) {
#pragma unroll
        for (int r = 0; r < 4; ++r) { y[r] = fmaf(acc0[4 * r4 + r], rstd, yo[r4][r]); PIN(y[r]); }
      } else if (st == 1) {
#pragma unroll
        for (int r = 0; r < 3; ++r) { u1[r] = __builtin_amdgcn_exp2f(y[r]); PIN(u1[r]); }
      } else if (st == 2) {
        u1[3] = __builtin_amdgcn_exp2f(y[3]); PIN(u1[3]);
#pragma unroll
        for (int r = 0; r < 4; ++r) { a1[r] = fmaf(u1[r], 0.5f, 1.f); PIN(a1[r]); }
      } else if (st == 3) {
#pragma unroll
        for (int r = 0; r < 4; ++r) { u1[r] = fmaf(u1[r], a1[r], 1.f); PIN(u1[r]); }
        u1[0] = __builtin_amdgcn_rcpf(u1[0]); PIN(u1[0]);
      } else if (st == 4) {
#pragma unroll
        for (int r = 1; r < 4; ++r) { u1[r] = __builtin_amdgcn_rcpf(u1[r]); PIN(u1[r]); }
      } else {
#pragma unroll
        for (int r = 0; r < 4; ++r) { y[r] = fmaf(-y[r], u1[r], y[r]); PIN(y[r]); }
        o[2 * (r4 & 1)] = pk(y[0], y[1]); o[2 * (r4 & 1) + 1] = pk(y[2], y[3]);
        PIN(o[2 * (r4 & 1)]); PIN(o[2 * (r4 & 1) + 1]);
        if (r4 & 1) __builtin_amdgcn_raw_buffer_store_b128(v4u{o[0], o[1], o[2], o[3]}, rw, ao + (r4 >> 1) * 1024, 0, SC1);
      }
    };
    if (SEQ) mish16(acc0, rstd, ao, PK1);
    ST(19);
#pragma unroll
    for (int s = 0; s < 32; ++s) {
      if (s == 13) { STX(55); if (SEQ && FL2) WAITV(2); else if (SEQ) set_flag(rw, W_FA + ma * 8192 + fo, tag, lane); else WAITV(1); STX(56); BAR(); STX(57); }
      if (s == 21) { if (SEQ && FL2) { WAITV(4); if (lane < 8) __builtin_amdgcn_raw_buffer_store_b128(v4u{tag, tag, tag, tag}, rw, W_FA + ma * 8192 + fo + lane * 16, 0, SC1); } BAR(); }
      if (!SEQ && s == F1S) {
        WAITV(4);
        if (lane < 8) __builtin_amdgcn_raw_buffer_store_b128(v4u{tag, tag, tag, tag}, rw, W_FA + ma * 8192 + fo + lane * 16, 0, SC1);
      }
      MMA32(acc1, w1f[s], hr[s & 3]);
      if (s >= 12 && s < 20) wp_[(s - 12) >> 1][s & 1] = trd2(((s & 1) ? pK0 : pS0) + ((s - 12) >> 1) * 2048, 1024);
      if (s + 3 < 32) hr[(s + 3) & 3] = *(LAS v4u*)(hp + ((s + 3) >> 3) * UB + ((s + 3) & 7) * 1024);
      feed(32 + s);
      if (!SEQ && s < 21) {
#pragma unroll
        for (int t = (s * 8) / 7; t < ((s + 1) * 8) / 7; ++t) epi(t % 6, t / 6);
      }
      offs(s, mr2);
      SCHED();
    }
    ST(11);
    if (SEQ && !G0) asm volatile("buffer_load_dword %0, %1, %2, 0 offen sc1" : "=v"(ef) : "v"(efo), "s"(rw) : "memory");
    mish16(acc1, rstd2, ao2, PK2);
    if (!SEQ && !G0) asm volatile("buffer_load_dword %0, %1, %2, 0 offen sc1" : "=v"(ef) : "v"(efo), "s"(rw) : "memory");
    ST(18);
    fin = W_FA + mb * 8192 + fo;
  }
  if (G0) {
    set_flag(rw, fin, tag, lane);
    ST(12);
    LBAR();
    if (w >= 2) {
      w2_unit2(8);
#pragma unroll 1
      for (int u = 10; u < 9 + NPRE; ++u) w2_unit2(u);
    }
    if ((SPOLL & 8) && w < 2) {
      __builtin_amdgcn_s_sleep(DLYL);
      wait_flags_s<14>(ws + W_FA + (2 * i + w) * 8192 + ((i & 1) ^ 1) * 4096, tag);
      wait_flags_s<14>(ws + W_FA + (2 * i + w) * 8192 + ((i & 1) ^ 1) * 4096 + 2048, tag);
    }
    if (!(SPOLL & 8) && w == 0) {
      __builtin_amdgcn_s_sleep(DLYL);
      wait_flags(rw, W_FA + (2 * i + hi) * 8192 + ((((i & 1) ^ 1) * 2 + (g & 1)) * 16 + (c < 14 ? c : 0)) * 128, tag);
    }
    ST(15);
    LBAR();
    WAITV(8 * (NPRE - 1)); LBAR();
    if (w >= 2) {
#pragma unroll 1
      for (int u = 9 + NPRE; u < 14; ++u) w2_unit2(u);
    }
    WAITV(8 * (5 - NPRE)); LBAR();
    WAITV(0); LBAR();
    ST(17); ST_DUMP();
    return;
  }
  {
    constexpr int LA = DLA;
    __builtin_amdgcn_s_setprio(1);
    int ae = W_ACT + (m32b * 112 + 8 * (i & 1) + 4 * kh) * 1024 + lane * 16, al = ae ^ 8192;
    PIN(ae); PIN(al);
    if (!SEQ) {
#pragma unroll
      for (int n = 0; n < 4 * NPRE - 4; ++n) {
        wp_[4 + n][0] = trd2(pS0 + ((4 + n) >> 2) * UB + (n & 3) * 2048, 1024);
        wp_[4 + n][1] = trd2(pK0 + ((4 + n) >> 2) * UB + (n & 3) * 2048, 1024);
      }
      SCHED();
      asm volatile("s_waitcnt vmcnt(0)" : "+v"(ef)::"memory");
      if (lane < 8) __builtin_amdgcn_raw_buffer_store_b128(v4u{tag, tag, tag, tag}, rw, fin + lane * 16, 0, SC1);
      ST(12);
      BAR();
    } else asm volatile("s_waitcnt vmcnt(2)" : "+v"(ef)::"memory");
    if (!__all(ef == tag)) {
      set_flag(rw, fin, tag, lane);
      wait_flags(rw, efo, tag);
    }
    asm volatile("" ::: "memory");
    ST(13);
    v4u af[LA];
    auto lda = [&](int n) {
      af[n % LA] = __builtin_amdgcn_raw_buffer_load_b128(rw, (n < 28 ? ae : al) + (n & 3) * 1024, upr((n % 28) >> 2) * 16384, SC1);
    };
#pragma unroll
    for (int n = 0; n < LA; ++n) {
      lda(n);
      if (SEQ && n < 4 * NPRE - 4) {
        wp_[4 + n][0] = trd2(pS0 + ((4 + n) >> 2) * UB + (n & 3) * 2048, 1024);
        wp_[4 + n][1] = trd2(pK0 + ((4 + n) >> 2) * UB + (n & 3) * 2048, 1024);
      }
      SCHED();
      if (SEQ && n == FLP) {
        WAITV(FLP + 1);
        if (lane < 8) __builtin_amdgcn_raw_buffer_store_b128(v4u{tag, tag, tag, tag}, rw, fin + lane * 16, 0, SC1);
        ST(12);
        BAR();
      }
    }
    lptr pS[3], pK[3];
    pS[0] = pS0; pK[0] = pK0;
#pragma unroll
    for (int t = 1; t < 3; ++t) { pS[t] = pS0 + t * 65536; pK[t] = pK0 + t * 65536; PIN(pS[t]); PIN(pK[t]); }
    f16v acc0 = z16, acc1;
#pragma unroll
    for (int e = 0; e < 8; ++e) { acc1[2 * e] = blo(hpk[e]) * LOG2E; acc1[2 * e + 1] = bhi(hpk[e]) * LOG2E; }
    lptr sp = L + 4 * UB + wl * 4096 + lane * 16, rp = L + 4 * UB + (wl ^ 1) * 4096 + lane * 16;
    int oo = (m32b * 32 + r32) * 1024 + j * 128 + kh * 64 + hi * 16;
    PIN(sp); PIN(rp); PIN(oo); PIN(acc1);
    const rsrc_t rout = __builtin_amdgcn_make_buffer_rsrc((void*)out, 0, 2048 * 1024, 0x00020000);
    v4u wf[3][2];
#pragma unroll
    for (int n = 0; n < 56; ++n) {
      const int t = n + 2, u = t >> 2, loc = u < 4 ? u : u == 4 || u == 13 ? 8 : u < 9 ? u - 1 : u - 9;
      const int wo_ = (loc & 3) * UB + (t & 3) * 2048;
      if (n == 28 - LA || t == 24 || t == 40 || t == 48) LBAR();
      if (n == 28) ST(14);
      MMA32(acc0, n < 4 * NPRE ? wp_[n % (4 * NPRE)][0] : wf[n % 3][0], af[n % LA]);
      if (t < 56 && t >= 4 * NPRE) wf[t % 3][0] = trd2(pS[loc >> 2] + wo_, 1024);
      SCHED();
      MMA32(acc1, n < 4 * NPRE ? wp_[n % (4 * NPRE)][1] : wf[n % 3][1], af[n % LA]);
      if (t < 56 && t >= 4 * NPRE) wf[t % 3][1] = trd2(pK[loc >> 2] + wo_, 1024);
      if (n + LA < 56) lda(n + LA);
      SCHED();
    }
    ST(16);
#pragma unroll
    for (int r4 = 0; r4 < 4; ++r4) *(LAS f4*)(sp + r4 * 1024) = f4{acc0[4 * r4], acc0[4 * r4 + 1], acc0[4 * r4 + 2], acc0[4 * r4 + 3]};
    BAR();
    unsigned o[8];
    f4 t[4];
#pragma unroll
    for (int r4 = 0; r4 < 4; ++r4) t[r4] = *(LAS f4*)(rp + r4 * 1024);
    SCHED();
#pragma unroll
    for (int r4 = 0; r4 < 4; ++r4) {
      const f4 v = (t[r4] + f4{acc1[4 * r4], acc1[4 * r4 + 1], acc1[4 * r4 + 2], acc1[4 * r4 + 3]}) * LN2;
      o[2 * r4] = pk(v[0], v[1]); o[2 * r4 + 1] = pk(v[2], v[3]);
    }
#pragma unroll
    for (int k = 0; k < 2; ++k) {
      const auto sx = __builtin_amdgcn_permlane32_swap(o[4 * k], o[4 * k + 2], false, false);
      const auto sy = __builtin_amdgcn_permlane32_swap(o[4 * k + 1], o[4 * k + 3], false, false);
      __builtin_amdgcn_raw_buffer_store_b128(v4u{sx[0], sy[0], sx[1], sy[1]}, rout, oo + k * 32, 0, 0);
    }
  }
  asm volatile(".Lfb_code_end:");
  ST(17); STR(63);
  ST_DUMP();
}

__global__ void tiny(char* ws) { if (threadIdx.x == 999) ws[0] = 1; }
extern "C" void kernel_launch(void* const* d_in, const int* in_sizes, int n_in, void* d_out, int out_size, void* d_ws,
                              size_t ws_size, hipStream_t stream) {
  fused_block<<<256, 512, 0, stream>>>((const char*)d_in[0], (const char*)d_in[1], (const char*)d_in[2], (const char*)d_in[4],
                                       (const char*)d_in[5], (const char*)d_in[6], (char*)d_out, (char*)d_ws);
}
```
